```python
import torch

def _softmax(x, axis=-1):
    return torch.softmax(x, dim=axis)
def _gelu(x):
    return torch.nn.functional.gelu(x, approximate="tanh")
def _silu(x):
    return torch.nn.functional.silu(x)
def _sigmoid(x):
    return torch.sigmoid(x)
def _layernorm(x, axis=-1, eps=1e-5):
    m = x.mean(dim=axis, keepdim=True)
    v = x.var(dim=axis, keepdim=True, unbiased=False)
    return (x - m) / torch.sqrt(v + eps)
def _scatter_add(tgt, idx, src, axis=0):
    return tgt.clone().index_add_(axis, idx, src)
def _segment_sum(src, seg, n):
    out = torch.zeros((n,) + src.shape[1:], dtype=src.dtype)
    return out.index_add_(0, seg, src)
def _pad(x, pw):
    flat = tuple(v for pair in reversed(pw) for v in pair)
    return torch.nn.functional.pad(x, flat)

kernel_name = 'parallel_sum__sliding_block_cached__glu_gated_geglu__decode'

def setup_inputs():

    _w = (lambda a: (lambda t: t.to(torch.bfloat16) if t.is_floating_point() else t)(torch.as_tensor(a)))
    def _A_setup():
        B, Sq, D = 1, 1, 1024
        Hq, Hkv, Sk, Dh = 32, 4, 32768, 128
        assert Hq % Hkv == 0, (Hq, Hkv)
        return {
            "x": _w((lambda *s: torch.randn(*s, dtype=torch.bfloat16) * 0.05)(B, Sq, D)),
            "k_cache": _w((lambda *s: torch.randn(*s, dtype=torch.bfloat16) * 1.6)(B, Hkv, Sk, Dh)),
            "v_cache": _w((lambda *s: torch.randn(*s, dtype=torch.bfloat16) * 0.8)(B, Hkv, Sk, Dh)),
            "wq": _w((lambda *s: torch.randn(*s, dtype=torch.bfloat16) * 1.6)(D, Hq * Dh) * (D ** -0.5)),
            "wo": _w((lambda *s: torch.randn(*s, dtype=torch.bfloat16) * 0.8)(Hq * Dh, D) * ((Hq * Dh) ** -0.5)),
        }
    _A_in = _A_setup()

    def _B_setup():
        B, S, Dm, Df = 1, 1, 1024, 3584
        return {
            "x": _w((lambda *s: torch.randn(*s, dtype=torch.bfloat16) * 0.05)(B, S, Dm)),
            "w_gate": _w((lambda *s: torch.randn(*s, dtype=torch.bfloat16) * 0.8)(Dm, Df) * (Dm ** -0.5)),
            "w_up": _w((lambda *s: torch.randn(*s, dtype=torch.bfloat16) * 0.8)(Dm, Df) * (Dm ** -0.5)),
            "w_down": _w((lambda *s: torch.randn(*s, dtype=torch.bfloat16) * 0.8)(Df, Dm) * (Df ** -0.5)),
        }
    _B_in = _B_setup()

    return {
        "A_x": _A_in["x"],
        "A_k_cache": _A_in["k_cache"],
        "A_v_cache": _A_in["v_cache"],
        "A_wq": _A_in["wq"],
        "A_wo": _A_in["wo"],
        "B_x": _B_in["x"],
        "B_w_gate": _B_in["w_gate"],
        "B_w_up": _B_in["w_up"],
        "B_w_down": _B_in["w_down"],
    }

def reference(A_x, A_k_cache, A_v_cache, A_wq, A_wo, B_x, B_w_gate, B_w_up, B_w_down):
    def _A_ref(x, k_cache, v_cache, wq, wo):
        B, Sq, D = x.shape
        Hq, Dh = 32, 128
        Hkv, Sk = k_cache.shape[1], k_cache.shape[2]
        W = min(4096, max(256, Sk // 4))
        rep = Hq // Hkv
        q = (lambda x, *d: x.permute(*d))((x @ wq).reshape(B, Sq, Hq, Dh), 0, 2, 1, 3)
        k = (lambda x, n, axis: torch.repeat_interleave(x, n, dim=axis))(k_cache[:, :, Sk - W:, :], rep, axis=1)
        v = (lambda x, n, axis: torch.repeat_interleave(x, n, dim=axis))(v_cache[:, :, Sk - W:, :], rep, axis=1)
        scores = torch.einsum("bhqd,bhkd->bhqk", q, k) * (Dh ** -0.5)
        p = _softmax(scores, axis=-1)
        o = torch.einsum("bhqk,bhkd->bhqd", p, v)
        return (lambda x, *d: x.permute(*d))(o, 0, 2, 1, 3).reshape(B, Sq, Hq * Dh) @ wo

    def _B_ref(x, w_gate, w_up, w_down):
        g = torch.einsum("bsd,df->bsf", x, w_gate)
        u = torch.einsum("bsd,df->bsf", x, w_up)
        h = _gelu(g) * u
        return torch.einsum("bsf,fd->bsd", h, w_down)

    x = A_x
    xn = _layernorm(x, axis=-1)
    return x + _A_ref(xn, A_k_cache, A_v_cache, A_wq, A_wo) + _B_ref(xn, B_w_gate, B_w_up, B_w_down)
```

```cpp
#include <hip/hip_runtime.h>
#include <hip/hip_bf16.h>
#include <stdint.h>

typedef unsigned short u16;
typedef unsigned v4u __attribute__((ext_vector_type(4)));

static constexpr int D = 1024, HQ = 32, HKV = 4, SK = 32768, DH = 128, W = 4096, DF = 3584;

__device__ __forceinline__ float bf2f(u16 b) { return __uint_as_float(((unsigned)b) << 16); }
__device__ __forceinline__ u16 f2bf(float f) { unsigned u = __float_as_uint(f); u += 0x7fffu + ((u >> 16) & 1u); return (u16)(u >> 16); }
__device__ __forceinline__ float rbf(float f) { return bf2f(f2bf(f)); }
__device__ __forceinline__ float lo(unsigned v) { return __uint_as_float(v << 16); }
__device__ __forceinline__ float hi(unsigned v) { return __uint_as_float(v & 0xffff0000u); }

__device__ void layernorm_to_lds(const u16* x, float* xn, float* red) {
  int t = threadIdx.x;
  float v[4]; float s = 0.f;
  for (int i = 0; i < 4; ++i) { v[i] = bf2f(x[t * 4 + i]); s += v[i]; }
  for (int o = 32; o; o >>= 1) s += __shfl_xor(s, o);
  if ((t & 63) == 0) red[t >> 6] = s;
  __syncthreads();
  float mean = (red[0] + red[1] + red[2] + red[3]) * (1.f / D);
  __syncthreads();
  float q = 0.f;
  for (int i = 0; i < 4; ++i) { float d = v[i] - mean; q += d * d; }
  for (int o = 32; o; o >>= 1) q += __shfl_xor(q, o);
  if ((t & 63) == 0) red[t >> 6] = q;
  __syncthreads();
  float var = (red[0] + red[1] + red[2] + red[3]) * (1.f / D);
  float mb = rbf(mean), vb = rbf(var);
  float den = rbf(sqrtf(rbf(vb + 1e-5f)));
  for (int i = 0; i < 4; ++i) xn[t * 4 + i] = rbf(rbf(v[i] - mb) / den);
  __syncthreads();
}

__global__ void __launch_bounds__(256) k1(const u16* x, const u16* wq, const u16* wg, const u16* wu, float* q, float* h) {
  __shared__ float xn[D]; __shared__ float red[4]; __shared__ float part[2][32][64];
  layernorm_to_lds(x, xn, red);
  int t = threadIdx.x, c8 = t & 7, r = t >> 3;
  if (blockIdx.x < 64) {
    int c0 = blockIdx.x * 64 + c8 * 8;
    float acc[8] = {0};
    for (int k = r; k < D; k += 32) {
      v4u w = *(const v4u*)(wq + (size_t)k * 4096 + c0); float xv = xn[k];
      for (int j = 0; j < 4; ++j) { acc[2*j] += xv * lo(w[j]); acc[2*j+1] += xv * hi(w[j]); }
    }
    for (int j = 0; j < 8; ++j) part[0][r][c8 * 8 + j] = acc[j];
    __syncthreads();
    if (t < 64) { float s = 0; for (int i = 0; i < 32; ++i) s += part[0][i][t]; q[blockIdx.x * 64 + t] = rbf(s); }
  } else {
    int b = blockIdx.x - 64; int c0 = b * 64 + c8 * 8;
    float ag[8] = {0}, au[8] = {0};
    for (int k = r; k < D; k += 32) {
      v4u g = *(const v4u*)(wg + (size_t)k * DF + c0); v4u u = *(const v4u*)(wu + (size_t)k * DF + c0); float xv = xn[k];
      for (int j = 0; j < 4; ++j) { ag[2*j] += xv * lo(g[j]); ag[2*j+1] += xv * hi(g[j]); au[2*j] += xv * lo(u[j]); au[2*j+1] += xv * hi(u[j]); }
    }
    for (int j = 0; j < 8; ++j) { part[0][r][c8 * 8 + j] = ag[j]; part[1][r][c8 * 8 + j] = au[j]; }
    __syncthreads();
    if (t < 64) { float g = 0, u = 0; for (int i = 0; i < 32; ++i) { g += part[0][i][t]; u += part[1][i][t]; }
      g = rbf(g); u = rbf(u);
      float ge = 0.5f * g * (1.f + tanhf(0.7978845608028654f * (g + 0.044715f * g * g * g)));
      h[b * 64 + t] = rbf(rbf(ge) * u); }
  }
}

__global__ void __launch_bounds__(256) k2(const u16* kc, const u16* vc, const float* q, float* po, float* pml) {
  __shared__ float qs[8][128]; __shared__ float sc[8][64];
  int g = blockIdx.x >> 6, s = blockIdx.x & 63, t = threadIdx.x;
  for (int i = t; i < 1024; i += 256) qs[i >> 7][i & 127] = q[g * 1024 + i];
  __syncthreads();
  const u16* kb = kc + ((size_t)g * SK + (SK - W) + s * 64) * DH;
  const u16* vb = vc + ((size_t)g * SK + (SK - W) + s * 64) * DH;
  for (int i = t; i < 512; i += 256) { int key = i & 63, hh = i >> 6; float a = 0;
    for (int d = 0; d < 128; ++d) a += qs[hh][d] * bf2f(kb[key * 128 + d]);
    sc[hh][key] = rbf(rbf(a) * 0.08838834764831845f); }
  __syncthreads();
  if (t < 8) { float m = -INFINITY; for (int k = 0; k < 64; ++k) m = fmaxf(m, sc[t][k]); float l = 0;
    for (int k = 0; k < 64; ++k) { float p = __expf(sc[t][k] - m); sc[t][k] = p; l += p; }
    pml[((g * 8 + t) * 64 + s) * 2] = m; pml[((g * 8 + t) * 64 + s) * 2 + 1] = l; }
  __syncthreads();
  for (int i = t; i < 1024; i += 256) { int hh = i >> 7, d = i & 127; float a = 0;
    for (int k = 0; k < 64; ++k) a += sc[hh][k] * bf2f(vb[k * 128 + d]);
    po[((g * 8 + hh) * 64 + s) * 128 + d] = a; }
}

__global__ void __launch_bounds__(128) k3(const u16* wo, const u16* wd, const float* po, const float* pml, const float* h, float* fin) {
  __shared__ float vec[16];
  int b = blockIdx.x, t = threadIdx.x; const u16* wbase;
  if (b < 256) {
    int hh = b >> 3, d0 = (b & 7) * 16;
    if (t < 16) { float M = -INFINITY; for (int s = 0; s < 64; ++s) M = fmaxf(M, pml[(hh * 64 + s) * 2]);
      float L = 0, o = 0; for (int s = 0; s < 64; ++s) { float w = __expf(pml[(hh * 64 + s) * 2] - M); L += w * pml[(hh * 64 + s) * 2 + 1]; o += w * po[(hh * 64 + s) * 128 + d0 + t]; }
      vec[t] = rbf(o / L); }
    wbase = wo + (size_t)b * 16 * D;
  } else { if (t < 16) vec[t] = h[(b - 256) * 16 + t]; wbase = wd + (size_t)(b - 256) * 16 * D; }
  __syncthreads();
  float acc[8] = {0};
  for (int r = 0; r < 16; ++r) { v4u w = *(const v4u*)(wbase + r * D + t * 8); float xv = vec[r];
    for (int j = 0; j < 4; ++j) { acc[2*j] += xv * lo(w[j]); acc[2*j+1] += xv * hi(w[j]); } }
  for (int j = 0; j < 8; ++j) fin[(size_t)b * D + t * 8 + j] = acc[j];
}

__global__ void __launch_bounds__(256) k4(const u16* x, const float* fin, u16* out) {
  __shared__ float pa[8][32], pb[8][32];
  int c = blockIdx.x * 32 + (threadIdx.x & 31), r = threadIdx.x >> 5;
  float a = 0, bsum = 0;
  for (int i = r; i < 256; i += 8) a += fin[(size_t)i * D + c];
  for (int i = 256 + r; i < 480; i += 8) bsum += fin[(size_t)i * D + c];
  pa[r][threadIdx.x & 31] = a; pb[r][threadIdx.x & 31] = bsum;
  __syncthreads();
  if (threadIdx.x < 32) { float A = 0, B = 0; for (int i = 0; i < 8; ++i) { A += pa[i][threadIdx.x]; B += pb[i][threadIdx.x]; }
    out[c] = f2bf(rbf(bf2f(x[c]) + rbf(A)) + rbf(B)); }
}

extern "C" void kernel_launch(void* const* d_in, const int* in_sizes, int n_in, void* d_out, int out_size, void* d_ws, size_t ws_size, hipStream_t stream) {
  const u16* x = (const u16*)d_in[0]; const u16* kc = (const u16*)d_in[1]; const u16* vc = (const u16*)d_in[2];
  const u16* wq = (const u16*)d_in[3]; const u16* wo = (const u16*)d_in[4];
  const u16* wg = (const u16*)d_in[6]; const u16* wu = (const u16*)d_in[7]; const u16* wd = (const u16*)d_in[8];
  float* ws = (float*)d_ws;
  float* q = ws; float* h = ws + 4096; float* pml = ws + 8192; float* po = ws + 16384; float* fin = po + 32 * 64 * 128;
  k1<<<120, 256, 0, stream>>>(x, wq, wg, wu, q, h);
  k2<<<256, 256, 0, stream>>>(kc, vc, q, po, pml);
  k3<<<480, 128, 0, stream>>>(wo, wd, po, pml, h, fin);
  k4<<<32, 256, 0, stream>>>(x, fin, (u16*)d_out);
}
```

```cpp
#include <hip/hip_runtime.h>
#include <stdint.h>

typedef unsigned short u16;
typedef unsigned long long u64;
typedef unsigned v4u __attribute__((ext_vector_type(4)));
typedef unsigned v2u __attribute__((ext_vector_type(2)));
typedef float v4f __attribute__((ext_vector_type(4)));
typedef float v2f __attribute__((ext_vector_type(2)));
typedef short v4s __attribute__((ext_vector_type(4)));
typedef __bf16 v8bf __attribute__((ext_vector_type(8)));
typedef __bf16 v2bf __attribute__((ext_vector_type(2)));
#define LAS __attribute__((address_space(3)))
typedef __amdgpu_buffer_rsrc_t rsrc_t;

#define QPOS 99
#define WOE 3
#define WOM 0
#define HBA 0
#define HBB 2
#define PSLEEP 2
#define CODE_KB 10

constexpr int D = 1024, SK = 32768, WIN = 4096, DF = 3584;
constexpr unsigned TMASK = 0x7FFu;
constexpr unsigned SPIN_CAP = 20000u;
constexpr float LOG2E = 1.4426950408889634f;
constexpr unsigned WS_QP = 4096;
constexpr unsigned WS_QF = WS_QP + 4 * 8 * 4096;
constexpr unsigned WS_ML = WS_QF + 4096 * 4;
constexpr unsigned WS_GU = WS_ML + 32 * 32 * 128;
constexpr unsigned WS_OP = WS_GU + 112 * 2048;
constexpr unsigned WS_FP = WS_OP + 32 * 2 * 32 * 256;
constexpr unsigned WS_END = WS_FP + 2 * 120 * 2048;
constexpr unsigned WS_STAMP = 4u << 20;

constexpr int L_RED = 0;
constexpr int L_V = 8 * 129 * 16;
constexpr int L_Q = L_V + 8 * 4608;
constexpr int L_O = L_Q + 8 * 272;
constexpr int L_ML = L_O + 8 * 8 * 528;
constexpr int L_X = L_ML + 512;
constexpr int L_G = L_X + 1024;
constexpr int L_END = L_G + 32 * 272 + 256;

#define SB() __builtin_amdgcn_sched_barrier(0)
#define STAMP(i) do {} while (0)

__device__ __forceinline__ rsrc_t mk(const void* p, unsigned bytes) { return __builtin_amdgcn_make_buffer_rsrc((void*)p, 0, bytes, 0x00020000); }
__device__ __forceinline__ v4u ldnt(rsrc_t r, unsigned v, unsigned s) { return __builtin_amdgcn_raw_buffer_load_b128(r, v, s, 2); }
__device__ __forceinline__ v4u ldpl(rsrc_t r, unsigned v, unsigned s) { return __builtin_amdgcn_raw_buffer_load_b128(r, v, s, 0); }
__device__ __forceinline__ v4u ldsc(rsrc_t r, unsigned v, unsigned s) { return __builtin_amdgcn_raw_buffer_load_b128(r, v, s, 16); }
__device__ __forceinline__ void stsc(v4u d, rsrc_t r, unsigned v, unsigned s) { __builtin_amdgcn_raw_buffer_store_b128(d, r, v, s, 16); }
__device__ __forceinline__ void stsc1(unsigned d, rsrc_t r, unsigned v, unsigned s) { __builtin_amdgcn_raw_buffer_store_b32(d, r, v, s, 16); }
__device__ __forceinline__ float asf(unsigned u) { return __builtin_bit_cast(float, u); }
__device__ __forceinline__ unsigned asu(float f) { return __builtin_bit_cast(unsigned, f); }
__device__ __forceinline__ float blo(unsigned v) { return asf(v << 16); }
__device__ __forceinline__ float bhi(unsigned v) { return asf(v & 0xffff0000u); }
__device__ __forceinline__ unsigned pk(float a, float b) { v2f f = {a, b}; return __builtin_bit_cast(unsigned, __builtin_convertvector(f, v2bf)); }
__device__ __forceinline__ float rbf(float a) { return blo(pk(a, a)); }
__device__ __forceinline__ unsigned tagw(float v, unsigned tag) { return ((asu(v) + 0x400u) & ~TMASK) | tag; }
__device__ __forceinline__ v4u tag4(v4f v, unsigned tag) { return v4u{tagw(v[0], tag), tagw(v[1], tag), tagw(v[2], tag), tagw(v[3], tag)}; }
__device__ __forceinline__ unsigned bad4(v4u v, unsigned tag) { return ((v[0] ^ tag) | (v[1] ^ tag) | (v[2] ^ tag) | (v[3] ^ tag)); }
__device__ __forceinline__ bool allok(unsigned bad) { return __all((bad & TMASK) == 0); }
__device__ __forceinline__ float unt(unsigned w) { return asf(w & ~TMASK); }
__device__ __forceinline__ v4f unt4(v4u v) { return v4f{unt(v[0]), unt(v[1]), unt(v[2]), unt(v[3])}; }
template <int CTRL> __device__ __forceinline__ float dpp(float x) { return asf(__builtin_amdgcn_mov_dpp(asu(x), CTRL, 0xf, 0xf, true)); }
constexpr int ROR8 = 0x128, ROR4 = 0x124, ROR2 = 0x122, ROR1 = 0x121;
__device__ __forceinline__ float swap32_add(float a, float b) {
  auto r = __builtin_amdgcn_permlane32_swap(asu(a), asu(b), false, false);
  return asf(r[0]) + asf(r[1]);
}
__device__ __forceinline__ float swap16_add(float a, float b) {
  auto r = __builtin_amdgcn_permlane16_swap(asu(a), asu(b), false, false);
  return asf(r[0]) + asf(r[1]);
}
__device__ __forceinline__ float tr4(v4f v) { return swap16_add(swap32_add(v[0], v[2]), swap32_add(v[1], v[3])); }
__device__ __forceinline__ float swap32_max(float a) { auto r = __builtin_amdgcn_permlane32_swap(asu(a), asu(a), false, false); return fmaxf(asf(r[0]), asf(r[1])); }
__device__ __forceinline__ float swap16_max(float a) { auto r = __builtin_amdgcn_permlane16_swap(asu(a), asu(a), false, false); return fmaxf(asf(r[0]), asf(r[1])); }
__device__ __forceinline__ float wave_sum(float x) {
  x += dpp<ROR8>(x); x += dpp<ROR4>(x); x += dpp<ROR2>(x); x += dpp<ROR1>(x);
  x = swap16_add(x, x); x = swap32_add(x, x); return x;
}
__device__ __forceinline__ v4f mfma32(v4u a, v4u b, v4f c) {
  return __builtin_amdgcn_mfma_f32_16x16x32_bf16(__builtin_bit_cast(v8bf, a), __builtin_bit_cast(v8bf, b), c, 0, 0, 0);
}
__device__ __forceinline__ void bar() { asm volatile("s_waitcnt lgkmcnt(0)" ::: "memory"); __builtin_amdgcn_s_barrier(); asm volatile("" ::: "memory"); }
__device__ __forceinline__ void ldswait() { asm volatile("s_waitcnt lgkmcnt(0)" ::: "memory"); }
#define USE(v) asm volatile("" :: "v"(v))
#define PIN(v) asm volatile("" : "+v"(v))

template <int K> __device__ __forceinline__ void make_sel(int lane, v4u& se, v4u& so) {
  const int i = lane & 15, h = lane >> 4, j = i & 7;
  const bool valid = K == 4 ? ((i >> 3) == (h & 1)) : true;
  const unsigned e = (j & 1) ? 0x01000c0cu : 0x0c0c0100u, o = (j & 1) ? 0x03020c0cu : 0x0c0c0302u;
#pragma unroll
  for (int r = 0; r < 4; ++r) { const bool m = valid && (j >> 1) == r; se[r] = m ? e : 0x0c0c0c0cu; so[r] = m ? o : 0x0c0c0c0cu; }
}
__device__ __forceinline__ v4u diag(v4u xv, int i, v4u se, v4u so) {
  const unsigned d = xv[i >> 1]; const v4u s = (i & 1) ? so : se;
  return v4u{__builtin_amdgcn_perm(0u, d, s[0]), __builtin_amdgcn_perm(0u, d, s[1]), __builtin_amdgcn_perm(0u, d, s[2]), __builtin_amdgcn_perm(0u, d, s[3])};
}
__device__ __forceinline__ float ln_mean(v4u xs0, v4u xs1) {
  float s = 0.f;
#pragma unroll
  for (int r = 0; r < 4; ++r) s += (blo(xs0[r]) + bhi(xs0[r])) + (blo(xs1[r]) + bhi(xs1[r]));
  return wave_sum(s) * (1.f / D);
}
__device__ __forceinline__ v4u ln_apply(v4u xs0, v4u xs1, v4u xo, float mean) {
  float q = 0.f;
#pragma unroll
  for (int r = 0; r < 4; ++r) {
    float a = blo(xs0[r]) - mean, b = bhi(xs0[r]) - mean, c = blo(xs1[r]) - mean, d = bhi(xs1[r]) - mean;
    q += (a * a + b * b) + (c * c + d * d);
  }
  const float var = wave_sum(q) * (1.f / D);
  const float mb = rbf(mean);
  const float den = rbf(__builtin_amdgcn_sqrtf(rbf(rbf(var) + 1e-5f)));
  const float inv = __builtin_amdgcn_rcpf(den);
  v4u o;
#pragma unroll
  for (int r = 0; r < 4; ++r) { const unsigned t = pk(blo(xo[r]) - mb, bhi(xo[r]) - mb); o[r] = pk(blo(t) * inv, bhi(t) * inv); }
  return o;
}
__device__ __forceinline__ void gemv2_publish(v4f acc, int lane, unsigned tag, rsrc_t rws, unsigned off) {
  v4f o;
#pragma unroll
  for (int r = 0; r < 4; ++r) {
    const float t = dpp<ROR8>(acc[r]);
    auto s2 = __builtin_amdgcn_permlane32_swap(asu(t), asu(t), false, false);
    o[r] = acc[r] + asf(s2[1]);
  }
  if ((lane & 40) == 0) stsc(tag4(o, tag), rws, off, 0);
}

__global__ void __launch_bounds__(512, 2)
fused_layer(const u16* x, const u16* kc, const u16* vc, const u16* wq, const u16* wo, const u16* wg, const u16* wu, const u16* wd,
            u16* out, unsigned* ws) {
  __shared__ __attribute__((aligned(16))) char smem[L_END];
  LAS char* L = (LAS char*)smem;
  const int b = blockIdx.x, xl = b & 7, j = b >> 3;
  const int tid = threadIdx.x, lane = tid & 63, w = __builtin_amdgcn_readfirstlane(tid >> 6);
  u64 pc; asm volatile("s_getpc_b64 %0" : "=s"(pc));
  STAMP(0);
  if (j >= 30) {
    if (CODE_KB) {
      const rsrc_t rc = mk((const void*)(pc & ~255ull), CODE_KB * 1024);
      v4u c0 = ldpl(rc, ((j - 30) * 8 + w) * 1024 + lane * 16, 0);
      USE(c0);
    }
  }
  asm volatile("" : "+s"(x), "+s"(kc), "+s"(vc), "+s"(wq), "+s"(wo), "+s"(wg), "+s"(wu), "+s"(wd), "+s"(out), "+s"(ws));
  const rsrc_t rx = mk(x, D * 2), rws = mk(ws, WS_END);
  if (j >= 30) {
    const int r = xl * 2 + (j - 30);
    const unsigned tag = (__builtin_amdgcn_raw_buffer_load_b32(rws, 0, 0, 16) + 1u) & TMASK;
    const unsigned pb = WS_QP + (((r >> 2) * 8 + w) * 1024 + (r & 3) * 256) * 4 + lane * 16;
    const unsigned ob = WS_QF + (r * 256 + w * 32 + (lane & 7) * 4 + (lane >> 5) * 2 + ((lane >> 4) & 1)) * 4;
    LAS v4f* red = (LAS v4f*)(L + L_RED);
    v4u P;
#pragma nounroll
    for (unsigned spin = 0;; ++spin) {
      asm volatile("" ::: "memory");
      P = ldsc(rws, pb, 0);
      if (allok(bad4(P, tag))) break;
      if (spin > SPIN_CAP) { P[0] = 0x7fc00000u; break; }
      __builtin_amdgcn_s_sleep(PSLEEP);
    }
    STAMP(4);
    red[w * 129 + lane] = unt4(P);
    bar();
    float t = tr4(red[(lane >> 3) * 129 + w * 8 + (lane & 7)]);
    t += dpp<ROR8>(t);
    if ((lane & 8) == 0) stsc1((pk(t, t) & 0xffff0000u) | tag, rws, ob, 0);
    STAMP(9);
    return;
  }
  v4u xs0 = ldpl(rx, lane * 32, 0), xs1 = ldpl(rx, lane * 32 + 16, 0);
  unsigned epoch = __builtin_amdgcn_raw_buffer_load_b32(rws, 0, 0, 16);
  unsigned fail = 0;
  const int b5 = lane >> 5, b4 = (lane >> 4) & 1;
  const int cq = b5 * 32 + (lane & 15) * 2 + b4;

  if (j < 16) {
    const int a = xl * 16 + j, g = a >> 5, s = a & 31;
    const int cgi = s & 3, part = s >> 2;
    const int row0 = part * 128 + w * 16 + b5 * 8;
    const v4u xo = ldpl(rx, row0 * 2, 0);
    const int fcol = a * 8 + (lane & 1) * 4 + b5 * 2 + b4;
    const unsigned xcol = __builtin_amdgcn_raw_buffer_load_b16(rx, fcol * 2, 0, 0);
    const rsrc_t rq = mk(wq, D * 4096 * 2), rk = mk(kc, 4u * SK * 256), rv = mk(vc, 4u * SK * 256), ro = mk(wo, 4096 * D * 2);
    const unsigned qoff = row0 * 8192 + (g * 1024 + cgi * 256) * 2 + (lane & 31) * 16;
    const unsigned kvb = (unsigned)(g * SK + (SK - WIN) + s * 128 + w * 16) * 256u;
    const unsigned koff = kvb + (lane & 15) * 256 + (lane >> 4) * 16, voff = kvb + lane * 16;
    const int hh = g * 8 + (s >> 2), dh = (s >> 1) & 1, ch = s & 1;
    const unsigned ooff = (hh * 128 + dh * 64 + (lane >> 3) * 8) * 2048 + ch * 1024 + w * 128 + (lane & 7) * 16;
    const unsigned qb = WS_QF + (g * 1024 + w * 128) * 4 + (lane & 31) * 16;
    v4u Q[8], Kf[4], Vr[4], Wo[8], P;
#define WO_LD(i) Wo[i] = ldnt(ro, ooff, (i) * 2048)
#define A_ISSUE(n) do { SB(); if ((n) == QPOS) P = ldsc(rws, qb, 0); \
      if ((n) < 8) Q[(n) & 7] = ldnt(rq, qoff, ((n) & 7) * 8192); else if ((n) < 8 + WOE) WO_LD(((n) - 8) & 7); \
      else if ((n) < 12 + WOE) Kf[((n) - WOE) & 3] = ldnt(rk, koff, (((n) - WOE) & 3) * 64); \
      else if ((n) < 16 + WOE) Vr[((n) - WOE) & 3] = ldnt(rv, voff, (((n) - WOE) & 3) * 1024); else if ((n) < 16 + WOE + WOM) WO_LD(((n) - 16) & 7); SB(); } while (0)
#pragma unroll
    for (int n = 0; n < 5; ++n) { SB(); __builtin_amdgcn_s_barrier(); A_ISSUE(n); }
    asm volatile("" : "+v"(xs0), "+v"(xs1), "+v"(epoch));
    A_ISSUE(5);
    const unsigned tag = (epoch + 1u) & TMASK;
    v4u se, so; make_sel<4>(lane, se, so);
    float mean = ln_mean(xs0, xs1);
    PIN(mean);
    A_ISSUE(6);
    const v4u xn = ln_apply(xs0, xs1, xo, mean);
    v4f acc = {0.f, 0.f, 0.f, 0.f};
#pragma unroll
    for (int i = 0; i < 8; ++i) { acc = mfma32(diag(xn, i, se, so), Q[i], acc); if (i >= 2) A_ISSUE(5 + i); }
    STAMP(2);
    {
      LAS v4f* red = (LAS v4f*)(L + L_RED);
      red[w * 129 + cq] = acc;
      bar();
      float t = tr4(red[(lane >> 3) * 129 + w * 8 + (lane & 7)]);
      t += dpp<ROR8>(t);
      if ((lane & 8) == 0) stsc1(tagw(t, tag), rws, WS_QP + ((g * 8 + part) * 1024 + cgi * 256 + w * 32 + (lane & 7) * 4 + b5 * 2 + b4) * 4, 0);
    }
    STAMP(3);
    v4u se1, so1; make_sel<1>(lane, se1, so1);
    PIN(se1); PIN(so1);
#pragma unroll
    for (int u = 0; u < WOE; ++u) { USE(Wo[u]); A_ISSUE(13 + u); }
#pragma unroll
    for (int u = 0; u < 4; ++u) { USE(Kf[u]); A_ISSUE(13 + WOE + u); }
    STAMP(11);
    v4s Vt[8];
    {
      LAS char* vb = L + L_V + w * 4608;
#pragma unroll
      for (int u = 0; u < 4; ++u) { *(LAS v4u*)(vb + (4 * u + (lane >> 4)) * 288 + (lane & 15) * 16) = Vr[u]; A_ISSUE(17 + WOE + u); }
      ldswait();
      LAS char* tb = vb + ((lane >> 4) * 4 + ((lane >> 2) & 3)) * 288 + (lane & 3) * 8;
#pragma unroll
      for (int t = 0; t < 8; ++t) Vt[t] = __builtin_amdgcn_ds_read_tr16_b64_v4i16((LAS v4s*)(tb + t * 32));
    }
    STAMP(4);
    if (QPOS > 20 + WOE) P = ldsc(rws, qb, 0);
    if (__builtin_expect(!allok(bad4(P, tag)), 0)) {
#pragma nounroll
      for (unsigned spin = 0;; ++spin) {
        asm volatile("" ::: "memory");
        P = ldsc(rws, qb, 0);
        if (allok(bad4(P, tag))) break;
        if (spin > SPIN_CAP) { fail = 1; break; }
      }
    }
    constexpr int WN = WOE + WOM, WA = (WN + 5 < 8 - HBA) ? WN + 5 : 8 - HBA;
    SB();
#pragma unroll
    for (int i = WN; i < WA; ++i) WO_LD(i);
    SB();
    if (lane < 32) *(LAS v2u*)(L + L_Q + w * 272 + lane * 8) = v2u{__builtin_amdgcn_perm(P[1], P[0], 0x07060302u), __builtin_amdgcn_perm(P[3], P[2], 0x07060302u)};
    STAMP(5);
    bar();
    v4f sc = {0.f, 0.f, 0.f, 0.f};
    {
      LAS char* qp = L + L_Q + (lane & 7) * 272 + (lane >> 4) * 16;
#pragma unroll
      for (int u = 0; u < 4; ++u) sc = mfma32(Kf[u], *(LAS v4u*)(qp + u * 64), sc);
    }
    float m, l; v4s Pb;
    {
      const unsigned s01 = pk(sc[0], sc[1]), s23 = pk(sc[2], sc[3]);
      const float SCALE = 0.08838834764831845f;
      const unsigned t01 = pk(blo(s01) * SCALE, bhi(s01) * SCALE), t23 = pk(blo(s23) * SCALE, bhi(s23) * SCALE);
      const float s0 = blo(t01), s1 = bhi(t01), s2 = blo(t23), s3 = bhi(t23);
      m = fmaxf(fmaxf(s0, s1), fmaxf(s2, s3));
      m = swap32_max(swap16_max(m));
      const float p0 = __builtin_amdgcn_exp2f((s0 - m) * LOG2E), p1 = __builtin_amdgcn_exp2f((s1 - m) * LOG2E);
      const float p2 = __builtin_amdgcn_exp2f((s2 - m) * LOG2E), p3 = __builtin_amdgcn_exp2f((s3 - m) * LOG2E);
      Pb = __builtin_bit_cast(v4s, v2u{pk(p0, p1), pk(p2, p3)});
      l = (p0 + p1) + (p2 + p3);
    }
    {
      LAS char* ob = L + L_O + (w * 8 + (lane & 7)) * 528 + (lane >> 4) * 16 + ((lane >> 3) & 1) * 256;
      v4f O[8];
#pragma unroll
      for (int t = 0; t < 8; ++t) O[t] = __builtin_amdgcn_mfma_f32_16x16x16bf16_1k(Vt[t], Pb, v4f{0.f, 0.f, 0.f, 0.f}, 0, 0, 0);
      l = swap16_add(l, l); l = swap32_add(l, l);
      if (lane < 16) *(LAS float*)(L + L_ML + (lane & 7) * 64 + (lane >> 3) * 32 + w * 4) = (lane < 8) ? m : l;
      const bool up = (lane & 8) != 0;
#pragma unroll
      for (int t = 0; t < 4; ++t) {
        v4f v;
#pragma unroll
        for (int r = 0; r < 4; ++r) v[r] = up ? O[t + 4][r] : O[t][r];
        *(LAS v4f*)(ob + t * 64) = v;
      }
    }
    bar();
    STAMP(6);
    {
      const v4f m0 = *(LAS v4f*)(L + L_ML + w * 64), m1 = *(LAS v4f*)(L + L_ML + w * 64 + 16);
      const v4f lw = *(LAS v4f*)(L + L_ML + w * 64 + 32 + b5 * 16);
      v4f pp[4];
#pragma unroll
      for (int i = 0; i < 4; ++i) pp[i] = *(LAS v4f*)(L + L_O + ((b5 * 4 + i) * 8 + w) * 528 + (lane & 31) * 16);
      const float M = fmaxf(fmaxf(fmaxf(m0[0], m0[1]), fmaxf(m0[2], m0[3])), fmaxf(fmaxf(m1[0], m1[1]), fmaxf(m1[2], m1[3])));
      v4f o = {0.f, 0.f, 0.f, 0.f}; float Ls = 0.f;
#pragma unroll
      for (int i = 0; i < 4; ++i) {
        const float e = __builtin_amdgcn_exp2f(((b5 ? m1[i] : m0[i]) - M) * LOG2E);
        o += e * pp[i]; Ls += e * lw[i];
      }
#pragma unroll
      for (int r = 0; r < 4; ++r) o[r] = swap32_add(o[r], o[r]);
      Ls = swap32_add(Ls, Ls);
      const int head = g * 8 + w;
      const v4f ml = {M, Ls, M, Ls};
      const unsigned off = lane < 32 ? WS_OP + ((head * 2 + b4) * 32 + s) * 256 + (lane & 15) * 16 : WS_ML + (head * 32 + s) * 128;
      if (lane <= 32) stsc(tag4(lane < 32 ? o : ml, tag), rws, off, 0);
    }
    STAMP(7);
    SB();
#pragma unroll
    for (int i = WA; i < 8 - HBA; ++i) WO_LD(i);
    SB();
    {
      const int sp = w * 4 + (lane >> 4);
      const unsigned ob = WS_OP + ((hh * 2 + dh) * 32 + sp) * 256 + (lane & 15) * 16;
      const unsigned mb = WS_ML + (hh * 32 + sp) * 128;
      v4u PO, ML;
#pragma nounroll
      for (unsigned spin = 0;; ++spin) {
        asm volatile("" ::: "memory");
        PO = ldsc(rws, ob, 0); ML = ldsc(rws, mb, 0);
        if (allok(bad4(PO, tag) | bad4(ML, tag))) break;
        if (spin > SPIN_CAP) { fail = 1; break; }
        __builtin_amdgcn_s_sleep(PSLEEP);
      }
      SB();
#pragma unroll
      for (int i = 8 - HBA; i < 8; ++i) WO_LD(i);
      SB();
      if (fail) PO[0] = 0x7fc00000u;
      *(LAS v4f*)(L + L_G + sp * 272 + (lane & 15) * 16) = unt4(PO);
      if ((lane & 15) == 0) *(LAS v2f*)(L + L_G + 32 * 272 + sp * 8) = v2f{unt(ML[0]), unt(ML[1])};
      STAMP(10);
      bar();
      const v4f po = *(LAS v4f*)(L + L_G + (lane >> 1) * 272 + w * 32 + (lane & 1) * 16);
      const v2f ml = *(LAS v2f*)(L + L_G + 32 * 272 + (lane >> 1) * 8);
      float M = fmaxf(ml[0], dpp<ROR8>(ml[0])); M = fmaxf(M, dpp<ROR4>(M)); M = fmaxf(M, dpp<ROR2>(M)); M = swap32_max(swap16_max(M));
      const float e = __builtin_amdgcn_exp2f((ml[0] - M) * LOG2E);
      float Ls = e * ml[1];
      float o = tr4(e * po);
      Ls = swap32_add(Ls, Ls); Ls = swap16_add(Ls, Ls);
      o += dpp<ROR8>(o); Ls += dpp<ROR8>(Ls);
      o += dpp<ROR4>(o); Ls += dpp<ROR4>(Ls);
      o += dpp<ROR2>(o); Ls += dpp<ROR2>(Ls);
      o *= __builtin_amdgcn_rcpf(Ls);
      if ((lane & 14) == 0) *(LAS u16*)(L + L_X + (w * 8 + (lane & 1) * 4 + b5 * 2 + b4) * 2) = (u16)pk(o, o);
    }
    bar();
    STAMP(8);
    {
      const v4u ov = *(LAS v4u*)(L + L_X + (lane >> 3) * 16);
      v4f c0 = {0.f, 0.f, 0.f, 0.f}, c1 = {0.f, 0.f, 0.f, 0.f};
#pragma unroll
      for (int i = 0; i < 8; i += 2) { c0 = mfma32(diag(ov, i, se1, so1), Wo[i], c0); c1 = mfma32(diag(ov, i + 1, se1, so1), Wo[i + 1], c1); }
      gemv2_publish(c0 + c1, lane, tag, rws, WS_FP + (ch * 120 + (a >> 1)) * 2048 + (64 * w + 8 * (lane & 7) + 4 * b4) * 4);
    }
    STAMP(9);
    if (w == 0) {
      const unsigned fb = WS_FP + ((a >> 6) * 120 + (lane >> 1)) * 2048 + (a & 63) * 32 + (lane & 1) * 16;
      const bool last = lane < 48;
      v4u P0, P1, P2, P3;
#pragma nounroll
      for (unsigned spin = 0;; ++spin) {
        asm volatile("" ::: "memory");
        P0 = ldsc(rws, fb, 0); P1 = ldsc(rws, fb, 32 * 2048); P2 = ldsc(rws, fb, 64 * 2048); P3 = ldsc(rws, last ? fb : fb - 32 * 2048, 96 * 2048);
        const unsigned bad = bad4(P0, tag) | bad4(P1, tag) | bad4(P2, tag) | bad4(P3, tag);
        if (allok(bad)) break;
        if (spin > SPIN_CAP) { fail = 1; break; }
        __builtin_amdgcn_s_sleep(PSLEEP);
      }
      const v4f pa = unt4(P0) + unt4(P1);
      v4f pb = unt4(P2);
      if (last) pb += unt4(P3);
      float sa = tr4(pa), sb = tr4(pb);
      sa += dpp<ROR8>(sa); sb += dpp<ROR8>(sb);
      sa += dpp<ROR4>(sa); sb += dpp<ROR4>(sb);
      sa += dpp<ROR2>(sa); sb += dpp<ROR2>(sb);
      if ((lane & 14) == 0) {
        float r = rbf(blo(xcol) + rbf(sa)) + rbf(sb);
        if (fail) r = __builtin_nanf("");
        __builtin_amdgcn_raw_buffer_store_b16((u16)pk(r, r), mk(out, D * 2), fcol * 2, 0, 0);
      }
      if (a == 0 && lane == 0) stsc1(epoch + 1u, rws, 0, 0);
      STAMP(1);
    }
  } else {
    const int bi = xl * 14 + (j - 16), grp = bi >> 3, mi = bi & 7;
    const int row0 = mi * 128 + w * 16 + b5 * 8;
    const v4u xo = ldpl(rx, row0 * 2, 0);
    const rsrc_t rg = mk(wg, D * DF * 2), ru = mk(wu, D * DF * 2), rd = mk(wd, D * DF * 2);
    const unsigned goff = row0 * (DF * 2) + grp * 512 + (lane & 31) * 16;
    const int rq = mi >> 1, ch = mi & 1;
    const unsigned doff = (grp * 256 + rq * 64 + (lane >> 3) * 8) * 2048 + ch * 1024 + w * 128 + (lane & 7) * 16;
    v4u G[8], U[8], Dn[8];
#define B_ISSUE(n) do { SB(); if ((n) < 16) { if ((n) & 1) U[((n) >> 1) & 7] = ldnt(ru, goff, (((n) >> 1) & 7) * (DF * 2)); else G[((n) >> 1) & 7] = ldnt(rg, goff, (((n) >> 1) & 7) * (DF * 2)); } \
      else if ((n) < 24 - HBB) Dn[(n) & 7] = ldnt(rd, doff, ((n) & 7) * 2048); SB(); } while (0)
#pragma unroll
    for (int n = 0; n < 5; ++n) { SB(); __builtin_amdgcn_s_barrier(); B_ISSUE(n); }
    asm volatile("" : "+v"(xs0), "+v"(xs1), "+v"(epoch));
    STAMP(1);
    B_ISSUE(5);
    const unsigned tag = (epoch + 1u) & TMASK;
    v4u se, so; make_sel<4>(lane, se, so);
    float mean = ln_mean(xs0, xs1);
    PIN(mean);
    B_ISSUE(6);
    const v4u xn = ln_apply(xs0, xs1, xo, mean);
    v4f ag = {0.f, 0.f, 0.f, 0.f}, au = {0.f, 0.f, 0.f, 0.f};
#pragma unroll
    for (int i = 0; i < 8; ++i) {
      const v4u A = diag(xn, i, se, so);
      ag = mfma32(A, G[i], ag); if (i >= 1) B_ISSUE(5 + 2 * i);
      au = mfma32(A, U[i], au); if (i >= 1) B_ISSUE(6 + 2 * i);
    }
    STAMP(2);
    {
      LAS v4f* red = (LAS v4f*)(L + L_RED);
      red[w * 129 + cq] = ag; red[w * 129 + 64 + cq] = au;
      bar();
      float tg = tr4(red[(lane >> 3) * 129 + w * 8 + (lane & 7)]), tu = tr4(red[(lane >> 3) * 129 + 64 + w * 8 + (lane & 7)]);
      tg += dpp<ROR8>(tg); tu += dpp<ROR8>(tu);
      const bool up = (lane & 8) != 0;
      stsc1(tagw(up ? tu : tg, tag), rws, WS_GU + bi * 2048 + (w >> 1) * 512 + (up ? 256 : 0) + (w & 1) * 128 + ((lane & 7) * 4 + b5 * 2 + b4) * 4, 0);
    }
    STAMP(3);
    v4u se1, so1; make_sel<1>(lane, se1, so1);
    PIN(se1); PIN(so1);
#pragma unroll
    for (int u = 0; u < 3 - HBB; ++u) { USE(Dn[u]); B_ISSUE(21 + u); }
    {
      const unsigned gb = WS_GU + (grp * 8 + w) * 2048 + rq * 512 + (lane & 31) * 16;
      v4u PG;
#pragma nounroll
      for (unsigned spin = 0;; ++spin) {
        asm volatile("" ::: "memory");
        PG = ldsc(rws, gb, 0);
        if (allok(bad4(PG, tag))) break;
        if (spin > SPIN_CAP) { fail = 1; break; }
        __builtin_amdgcn_s_sleep(PSLEEP);
      }
      SB();
#pragma unroll
      for (int i = 8 - HBB; i < 8; ++i) Dn[i] = ldnt(rd, doff, i * 2048);
      SB();
      STAMP(4);
      LAS v4f* gl = (LAS v4f*)(L + L_G);
      if (fail) PG[0] = 0x7fc00000u;
      if (lane < 32) gl[w * 32 + lane] = unt4(PG);
      bar();
      const int pq = lane >> 4, qd = lane & 15;
      const float gs = tr4(gl[pq * 32 + qd] + gl[(pq + 4) * 32 + qd]), us = tr4(gl[pq * 32 + 16 + qd] + gl[(pq + 4) * 32 + 16 + qd]);
      const float gq = rbf(gs), uq = rbf(us);
      const float inner = 0.7978845608028654f * (gq + 0.044715f * gq * gq * gq);
      const float sg = __builtin_amdgcn_rcpf(1.f + __builtin_amdgcn_exp2f(inner * (-2.f * LOG2E)));
      const float hv = rbf(gq * sg) * uq;
      LAS char* hb = L + L_X + w * 128;
      *(LAS u16*)(hb + (qd * 4 + b5 * 2 + b4) * 2) = (u16)pk(hv, hv);
      ldswait();
      const v4u hvv = *(LAS v4u*)(hb + (lane >> 3) * 16);
      v4u A2[8];
#pragma unroll
      for (int i = 0; i < 8; ++i) A2[i] = diag(hvv, i, se1, so1);
      STAMP(5);
      SB();
      v4f c0 = {0.f, 0.f, 0.f, 0.f}, c1 = {0.f, 0.f, 0.f, 0.f};
#pragma unroll
      for (int i = 0; i < 8; i += 2) { c0 = mfma32(A2[i], Dn[i], c0); c1 = mfma32(A2[i + 1], Dn[i + 1], c1); }
      gemv2_publish(c0 + c1, lane, tag, rws, WS_FP + (ch * 120 + 64 + (bi >> 1)) * 2048 + (64 * w + 8 * (lane & 7) + 4 * b4) * 4);
    }
    STAMP(9);
  }
}

extern "C" void kernel_launch(void* const* d_in, const int* in_sizes, int n_in, void* d_out, int out_size, void* d_ws, size_t ws_size,
                              hipStream_t stream) {
  fused_layer<<<256, 512, 0, stream>>>((const u16*)d_in[0], (const u16*)d_in[1], (const u16*)d_in[2], (const u16*)d_in[3], (const u16*)d_in[4],
                                       (const u16*)d_in[6], (const u16*)d_in[7], (const u16*)d_in[8], (u16*)d_out, (unsigned*)d_ws);
}
```

```cpp
#include <hip/hip_runtime.h>
#include <stdint.h>

typedef unsigned short u16;
typedef unsigned long long u64;
typedef unsigned v4u __attribute__((ext_vector_type(4)));
typedef unsigned v2u __attribute__((ext_vector_type(2)));
typedef float v4f __attribute__((ext_vector_type(4)));
typedef float v2f __attribute__((ext_vector_type(2)));
typedef unsigned v16u __attribute__((ext_vector_type(16)));
typedef short v4s __attribute__((ext_vector_type(4)));
typedef __bf16 v8bf __attribute__((ext_vector_type(8)));
typedef __bf16 v2bf __attribute__((ext_vector_type(2)));
typedef __bf16 v16bf __attribute__((ext_vector_type(16)));
#define LAS __attribute__((address_space(3)))
typedef v4u rsrc_t;
__device__ v4u raw_ld128(v4u, unsigned, unsigned, int) __asm("llvm.amdgcn.raw.buffer.load.v4i32");
__device__ v2u raw_ld64(v4u, unsigned, unsigned, int) __asm("llvm.amdgcn.raw.buffer.load.v2i32");
__device__ unsigned raw_ld32(v4u, unsigned, unsigned, int) __asm("llvm.amdgcn.raw.buffer.load.i32");
__device__ u16 raw_ld16(v4u, unsigned, unsigned, int) __asm("llvm.amdgcn.raw.buffer.load.i16");
__device__ void raw_st128(v4u, v4u, unsigned, unsigned, int) __asm("llvm.amdgcn.raw.buffer.store.v4i32");
__device__ void raw_st64(v2u, v4u, unsigned, unsigned, int) __asm("llvm.amdgcn.raw.buffer.store.v2i32");
__device__ void raw_st32(unsigned, v4u, unsigned, unsigned, int) __asm("llvm.amdgcn.raw.buffer.store.i32");
__device__ void raw_st16(u16, v4u, unsigned, unsigned, int) __asm("llvm.amdgcn.raw.buffer.store.i16");

constexpr int WA = 4, WB = 4;
constexpr int QAT = 5;
constexpr int WOB = 5;
constexpr int DPR = 6;
constexpr int PACE = 14, PT0 = 173, PTD = 25, TRS = 750;
constexpr int CODE_BYTES = 11488;

constexpr int D = 1024, SK = 32768, WIN = 4096, DF = 3584;
constexpr unsigned TMASK = 0x7FFu;
constexpr unsigned SPIN_CAP = 20000u;
constexpr unsigned OOB = 0x80000000u;
constexpr float LOG2E = 1.4426950408889634f;
constexpr unsigned WS_PH = 1024;
constexpr unsigned WS_QP = 4096;
constexpr unsigned WS_ML = (WS_QP + 32 * 9 * 512 + 16383) & ~16383u;
constexpr unsigned WS_PR = WS_ML + 16384;
constexpr unsigned WS_FP = WS_PR + 16384;
constexpr unsigned WS_GU = WS_FP + 30 * 16384;
constexpr unsigned WS_OP = WS_GU + 32 * 16384;
constexpr unsigned WS_END = WS_OP + 32 * 16384;

constexpr int L_RED = 0;
constexpr int L_V = 8 * 129 * 16;
constexpr int L_K = L_V + 128 * 288;
constexpr int L_Q = L_K + 128 * 272;
constexpr int L_P = L_Q + 8 * 272;
constexpr int L_ML = L_P + 8 * 272;
constexpr int L_X = L_ML + 8 * 48;
constexpr int L_END = L_X + 256;

#define SB() __builtin_amdgcn_sched_barrier(0)

__device__ __forceinline__ v4u ldnt(rsrc_t r, unsigned v, unsigned s) { return raw_ld128(r, v, s, 2); }
__device__ __forceinline__ v4u ldpl(rsrc_t r, unsigned v, unsigned s) { return raw_ld128(r, v, s, 0); }
__device__ __forceinline__ v4u ldsc(rsrc_t r, unsigned v, unsigned s) { return raw_ld128(r, v, s, 16); }
__device__ __forceinline__ v2u ldsc2(rsrc_t r, unsigned v, unsigned s) { return raw_ld64(r, v, s, 16); }
__device__ __forceinline__ void stsc(v4u d, rsrc_t r, unsigned v, unsigned s) { raw_st128(d, r, v, s, 16); }
__device__ __forceinline__ void stsc2(v2u d, rsrc_t r, unsigned v, unsigned s) { raw_st64(d, r, v, s, 16); }
__device__ __forceinline__ void stsc1(unsigned d, rsrc_t r, unsigned v, unsigned s) { raw_st32(d, r, v, s, 16); }
__device__ __forceinline__ float asf(unsigned u) { return __builtin_bit_cast(float, u); }
__device__ __forceinline__ unsigned asu(float f) { return __builtin_bit_cast(unsigned, f); }
__device__ __forceinline__ float blo(unsigned v) { return asf(v << 16); }
__device__ __forceinline__ float bhi(unsigned v) { return asf(v & 0xffff0000u); }
__device__ __forceinline__ unsigned pk(float a, float b) { v2f f = {a, b}; return __builtin_bit_cast(unsigned, __builtin_convertvector(f, v2bf)); }
__device__ __forceinline__ float rbf(float a) { return blo(pk(a, a)); }
__device__ __forceinline__ float fmx(float a, float b) { float r; asm("v_max_f32 %0, %1, %2" : "=v"(r) : "v"(a), "v"(b)); return r; }
__device__ __forceinline__ float fmx3(float a, float b, float c) { float r; asm("v_max3_f32 %0, %1, %2, %3" : "=v"(r) : "v"(a), "v"(b), "v"(c)); return r; }
__device__ __forceinline__ unsigned tagw(float v, unsigned tag) { return ((asu(v) + 0x400u) & ~TMASK) | tag; }
__device__ __forceinline__ v4u tag4(v4f v, unsigned tag) { return v4u{tagw(v[0], tag), asu(v[1]), tagw(v[2], tag), asu(v[3])}; }
__device__ __forceinline__ unsigned bad4(v4u v, unsigned tag) { return __builtin_amdgcn_bitop3_b32(v[0], v[2], tag, 0x7e); }
__device__ __forceinline__ bool allok(unsigned bad) { return __builtin_amdgcn_ballot_w64((bad & TMASK) != 0) == 0; }
__device__ __forceinline__ float unt(unsigned w) { return asf(w & ~TMASK); }
__device__ __forceinline__ v4f unt4(v4u v) { return v4f{unt(v[0]), asf(v[1]), unt(v[2]), asf(v[3])}; }
template <int CTRL> __device__ __forceinline__ float dpp(float x) { return asf(__builtin_amdgcn_mov_dpp(asu(x), CTRL, 0xf, 0xf, true)); }
constexpr int ROR8 = 0x128, ROR4 = 0x124, ROR2 = 0x122, ROR1 = 0x121;
__device__ __forceinline__ float swap32_add(float a, float b) {
  auto r = __builtin_amdgcn_permlane32_swap(asu(a), asu(b), false, false);
  return asf(r[0]) + asf(r[1]);
}
__device__ __forceinline__ float swap16_add(float a, float b) {
  auto r = __builtin_amdgcn_permlane16_swap(asu(a), asu(b), false, false);
  return asf(r[0]) + asf(r[1]);
}
__device__ __forceinline__ float tr4(v4f v) { return swap16_add(swap32_add(v[0], v[2]), swap32_add(v[1], v[3])); }
__device__ __forceinline__ float swap32_max(float a) { auto r = __builtin_amdgcn_permlane32_swap(asu(a), asu(a), false, false); return fmx(asf(r[0]), asf(r[1])); }
__device__ __forceinline__ float swap16_max(float a) { auto r = __builtin_amdgcn_permlane16_swap(asu(a), asu(a), false, false); return fmx(asf(r[0]), asf(r[1])); }
__device__ __forceinline__ float wave_sum(float x) {
  x += dpp<ROR8>(x); x += dpp<ROR4>(x); x += dpp<ROR2>(x); x += dpp<ROR1>(x);
  x = swap16_add(x, x); x = swap32_add(x, x); return x;
}
__device__ __forceinline__ v4f mfma32(v4u a, v4u b, v4f c) {
  return __builtin_amdgcn_mfma_f32_16x16x32_bf16(__builtin_bit_cast(v8bf, a), __builtin_bit_cast(v8bf, b), c, 0, 0, 0);
}
__device__ __forceinline__ void bar() { asm volatile("s_waitcnt lgkmcnt(0)" ::: "memory"); __builtin_amdgcn_s_barrier(); asm volatile("" ::: "memory"); }
__device__ __forceinline__ void ldswait() { asm volatile("s_waitcnt lgkmcnt(0)" ::: "memory"); }
#define OFFIF(c) ((c) ? OOB : 0u)
#define USE(v) asm volatile("" :: "v"(v))
#define PIN(v) asm volatile("" : "+v"(v))
template <class T> __device__ __forceinline__ T ldl(unsigned a) { return *(LAS T*)(size_t)a; }
template <class T> __device__ __forceinline__ void stl(unsigned a, T v) { *(LAS T*)(size_t)a = v; }

__device__ __forceinline__ void make_sel(int lane, bool valid, v4u& se, v4u& so) {
  const int j = lane & 7;
  const unsigned e = (j & 1) ? 0x01000c0cu : 0x0c0c0100u, o = (j & 1) ? 0x03020c0cu : 0x0c0c0302u;
#pragma unroll
  for (int r = 0; r < 4; ++r) { const bool m = valid && (j >> 1) == r; se[r] = m ? e : 0x0c0c0c0cu; so[r] = m ? o : 0x0c0c0c0cu; }
}
__device__ __forceinline__ v4u diag(v4u xv, int i, v4u se, v4u so) {
  const unsigned d = xv[i >> 1]; const v4u s = (i & 1) ? so : se;
  return v4u{__builtin_amdgcn_perm(0u, d, s[0]), __builtin_amdgcn_perm(0u, d, s[1]), __builtin_amdgcn_perm(0u, d, s[2]), __builtin_amdgcn_perm(0u, d, s[3])};
}
__device__ __forceinline__ float ln_mean(v4u xs0, v4u xs1) {
  float s = 0.f;
#pragma unroll
  for (int r = 0; r < 4; ++r) s += (blo(xs0[r]) + bhi(xs0[r])) + (blo(xs1[r]) + bhi(xs1[r]));
  return wave_sum(s) * (1.f / D);
}
__device__ __forceinline__ float ln_inv(v4u xs0, v4u xs1, float mean) {
  float q = 0.f;
#pragma unroll
  for (int r = 0; r < 4; ++r) {
    float a = blo(xs0[r]) - mean, b = bhi(xs0[r]) - mean, c = blo(xs1[r]) - mean, d = bhi(xs1[r]) - mean;
    q += (a * a + b * b) + (c * c + d * d);
  }
  const float var = wave_sum(q) * (1.f / D);
  return __builtin_amdgcn_rcpf(rbf(__builtin_amdgcn_sqrtf(rbf(rbf(var) + 1e-5f))));
}
__device__ __forceinline__ unsigned ln2(unsigned v, float mb, float inv) { const unsigned t = pk(blo(v) - mb, bhi(v) - mb); return pk(blo(t) * inv, bhi(t) * inv); }
__device__ __forceinline__ void gemv64(v4u xa, v4u xb, unsigned s0, unsigned s1, int idx, const v4u (&W)[8], unsigned tag, rsrc_t rws, unsigned off, v4f c) {
  v4u A[4];
#pragma unroll
  for (int p = 0; p < 4; ++p) A[p] = v4u{__builtin_amdgcn_perm(0u, xa[p], s0), __builtin_amdgcn_perm(0u, xa[p], s1), __builtin_amdgcn_perm(0u, xb[p], s0), __builtin_amdgcn_perm(0u, xb[p], s1)};
  SB();
#pragma unroll
  for (int p = 0; p < 4; ++p)
    c = __builtin_amdgcn_smfmac_f32_16x16x64_bf16(__builtin_bit_cast(v8bf, A[p]), __builtin_bit_cast(v16bf, __builtin_shufflevector(W[2 * p], W[2 * p + 1], 0, 1, 2, 3, 4, 5, 6, 7)), c, idx, 0, 0);
  auto t = __builtin_amdgcn_permlane32_swap(asu(c[0]), asu(c[2]), false, false);
  auto u = __builtin_amdgcn_permlane32_swap(asu(c[1]), asu(c[3]), false, false);
  stsc2(v2u{tagw(asf(t[0]) + dpp<ROR8>(asf(t[1])), tag), asu(asf(u[0]) + dpp<ROR8>(asf(u[1])))}, rws, off, 0);
}
#define SM_SETUP() \
  do { const unsigned h_ = b5 ? 0x0c0c0302u : 0x0c0c0100u; sm0 = (lane & 4) ? 0x0c0c0c0cu : h_; sm1 = (lane & 4) ? h_ : 0x0c0c0c0cu; smi = (lane & 3) * 0x1111; } while (0)
#define FP_OFF(ch, p) ((WS_FP + ((p) >> 2) * 16384 + (ch) * 8192 + ((p) & 3) * 2048 + (64 * w + 8 * c8 + 4 * b4 + 2 * b5) * 4) | OFFIF(lane & 8))
#define BAD2(v) __builtin_amdgcn_bitop3_b32((v)[0], (v)[1], tag, 0x7e)
#define RETRY(LOADS, BAD) \
  if (__builtin_expect(!allok(BAD), 0)) { \
    _Pragma("nounroll") for (unsigned spin = 0; spin < SPIN_CAP; ++spin) { \
      asm volatile("" ::: "memory"); LOADS; \
      if (allok(BAD)) break; \
      __builtin_amdgcn_s_sleep(1); } }

__global__ void __launch_bounds__(512, 2)
fused_layer(const u16*, const u16*, const u16*, const u16*, const u16*, const u16*, const u16*, const u16*, u16*, unsigned*) {
  __shared__ __attribute__((aligned(16))) char smem[L_END];
  const unsigned L = (unsigned)(size_t)(LAS char*)smem;
  int b = blockIdx.x, tid = threadIdx.x;
  v16u ka; v4u kb; v2u p1, p2;
  {
    const bool mlp = b >= 144;
    asm volatile("s_load_dwordx2 %2, %6, %7\n\ts_load_dwordx16 %0, %6, 0x0\n\ts_load_dwordx4 %1, %6, 0x40\n\ts_load_dwordx2 %3, %6, %8"
                 : "=&s"(ka), "=&s"(kb), "=&s"(p1), "=&s"(p2), "+s"(b), "+v"(tid)
                 : "s"(__builtin_amdgcn_kernarg_segment_ptr()), "s"(mlp ? 0x28 : 0x18), "s"(mlp ? 0x30 : 0x18));
  }
  int w = __builtin_amdgcn_readfirstlane(tid >> 6);
  u64 tc0, pc;
  asm volatile("s_memrealtime %0" : "=s"(tc0));
  asm volatile("s_getpc_b64 %0" : "=s"(pc));
  int xr0, cb, xsh, rsb, rmul, t0_, t1_;
  unsigned sz1, szx, szs, szc, tc_, o1, o4, o5, fmt = 0x00020000u, szw = WS_END, szd = D * 2;
  asm volatile(
      "s_mov_b32 %[szs], 0\n\t"
      "s_mov_b32 %[szc], 0\n\t"
      "s_mov_b32 %[tc], 0\n\t"
      "s_mov_b32 %[rmul], 8\n\t"
      "s_cmp_ge_u32 %[b], 144\n\t"
      "s_cbranch_scc1 1f\n\t"
      "s_mov_b32 %[xsh], 5\n\t"
      "s_movk_i32 %[rsb], 0x2000\n\t"
      "s_mov_b32 %[sz1], 0x800000\n\t"
      "s_mov_b32 %[szx], %[sz1]\n\t"
      "s_movk_i32 %[o1], 0x2000\n\t"
      "s_mov_b32 %[o4], 0x8000\n\t"
      "s_mov_b32 %[o5], 0xa000\n\t"
      "s_cmp_lt_u32 %[b], 16\n\t"
      "s_cbranch_scc1 2f\n\t"
      "s_sub_u32 %[t0], %[b], 16\n\t"
      "s_lshr_b32 %[t1], %[t0], 5\n\t"
      "s_and_b32 %[xr0], %[b], 1\n\t"
      "s_lshl2_add_u32 %[xr0], %[xr0], %[t1]\n\t"
      "s_lshl3_add_u32 %[xr0], %[xr0], %[w]\n\t"
      "s_mul_i32 %[xr0], %[xr0], 14\n\t"
      "s_bfe_u32 %[t0], %[t0], 0x20003\n\t"
      "s_and_b32 %[cb], %[b], 6\n\t"
      "s_lshl1_add_u32 %[cb], %[cb], %[t0]\n\t"
      "s_lshl_b32 %[cb], %[cb], 9\n\t"
      "s_mov_b32 %[rmul], 7\n\t"
      "s_branch 3f\n"
      "1:\n\t"
      "s_and_b32 %[t0], %[b], 7\n\t"
      "s_mul_i32 %[t0], %[t0], 14\n\t"
      "s_lshr_b32 %[t1], %[b], 3\n\t"
      "s_add_i32 %[t0], %[t0], %[t1]\n\t"
      "s_sub_u32 %[t0], %[t0], 18\n\t"
      "s_and_b32 %[xr0], %[t0], 15\n\t"
      "s_lshl_b32 %[xr0], %[xr0], 6\n\t"
      "s_lshr_b32 %[t0], %[t0], 4\n\t"
      "s_lshl3_add_u32 %[cb], %[t0], %[w]\n\t"
      "s_lshl_b32 %[cb], %[cb], 7\n\t"
      "s_mov_b32 %[xsh], 3\n\t"
      "s_movk_i32 %[rsb], 0x1c00\n\t"
      "s_mov_b32 %[sz1], 0x700000\n\t"
      "s_mov_b32 %[szx], 0\n\t"
      "s_mov_b32 %[o1], 0\n\t"
      "s_movk_i32 %[o4], 0x1c00\n\t"
      "s_movk_i32 %[o5], 0x1c00\n\t"
      "s_branch 3f\n"
      "2:\n\t"
      "s_lshl_b32 %[xr0], %[w], 4\n\t"
      "s_addk_i32 %[xr0], 896\n\t"
      "s_and_b32 %[t0], %[b], 7\n\t"
      "s_lshr_b32 %[t1], %[b], 3\n\t"
      "s_lshl1_add_u32 %[cb], %[t0], %[t1]\n\t"
      "s_lshl_b32 %[cb], %[cb], 9\n\t"
      "s_lshl3_add_u32 %[tc], %[t1], %[w]\n\t"
      "s_lshl_b32 %[tc], %[tc], 10\n\t"
      "s_movk_i32 %[szc], %[code]\n\t"
      "s_mov_b32 %[szs], %[sz1]\n"
      "3:"
      : [xr0] "=&s"(xr0), [cb] "=&s"(cb), [xsh] "=&s"(xsh), [rsb] "=&s"(rsb), [rmul] "=&s"(rmul), [sz1] "=&s"(sz1), [szx] "=&s"(szx), [szs] "=&s"(szs), [szc] "=&s"(szc), [tc] "=&s"(tc_),
        [o1] "=&s"(o1), [o4] "=&s"(o4), [o5] "=&s"(o5), [t0] "=&s"(t0_), [t1] "=&s"(t1_), "+s"(fmt), "+s"(szw), "+s"(szd)
      : [b] "s"(b), [w] "s"(w), [code] "n"(CODE_BYTES) : "scc");
  int lane = tid & 63;
  const v4u cpf = ldpl(rsrc_t{(unsigned)pc & ~255u, (unsigned)(pc >> 32), szc, fmt}, lane * 16, tc_);
  SB();
  const int xrow = xr0 + (lane >> xsh) * rmul;
  unsigned soff = xrow * rsb + cb + (lane & ((1 << xsh) - 1)) * 16;
  unsigned xoff = xrow * 2 & ~3, xso = lane * 32, xsf = xrow * 16;
  asm volatile("" : "+v"(soff), "+v"(xoff), "+v"(xso), "+v"(xsf));
  asm volatile("s_waitcnt lgkmcnt(0)" : "+s"(ka), "+s"(kb), "+s"(p1), "+s"(p2), "+s"(tc0));
  const rsrc_t r1 = {p1[0], p1[1], sz1, fmt};
  v4u S[16];
  SB(); S[0] = ldnt(r1, soff, 0); SB();
  const u64 wsa = ((((u64)kb[3] << 32) | kb[2]) + 0x1fffffu) & ~0x1fffffull;
  const rsrc_t rx = {ka[0], ka[1], szd, fmt}, rws = {(unsigned)wsa, (unsigned)(wsa >> 32), szw, fmt}, r2 = {p2[0], p2[1], sz1, fmt};
  v4u xs0 = ldpl(rx, xso, 0), xs1 = ldpl(rx, xso, 16);
  v4u xo = ldpl(rx, xoff, 0);
  unsigned epoch = raw_ld32(rws, 0, 0, 16);
  SB(); __builtin_amdgcn_s_barrier(); S[1] = ldnt(r2, soff, o1); SB();
  const rsrc_t r1x = {p1[0], p1[1], szx, fmt};
  __builtin_amdgcn_s_barrier(); const v4u T2 = ldnt(r1x, soff, 2 * 8192); SB();
  __builtin_amdgcn_s_barrier(); const v4u T3 = ldnt(r1x, soff, 3 * 8192); SB();
  const v4u T7 = ldnt(rsrc_t{p1[0], p1[1], szs, fmt}, soff, 7 * 8192); SB();
  asm volatile("" : "+v"(tid), "+v"(lane), "+s"(b), "+s"(w));
  __builtin_assume(tid >= 0 && tid < 512); __builtin_assume(lane >= 0 && lane < 64); __builtin_assume(b >= 0 && b < 256); __builtin_assume(w >= 0 && w < 8);
  const int xl = b & 7, jd = b >> 3, j = (jd + 30) & 31;
  const bool isA = j < 16, isS = j >= 30, isB = !isA && !isS;
  const int b5 = lane >> 5, b4 = (lane >> 4) & 1, hq = lane >> 4, n = lane & 15, c8 = lane & 7;
  const int e4 = b5 * 2 + b4;
  const int a = xl * 16 + j, g = a >> 5, s = a & 31;
  const int cgi = s & 3, part = s >> 2;
  const int bi = xl * 14 + j - 16, grp = bi >> 4, mi = bi & 15;
  const int r = xl * 2 + j - 30;
  const unsigned tz = (unsigned)tc0 - ((jd * 3) >> 3);
#define AT(T) do { const unsigned tt_ = tz + (T); \
      for (;;) { u64 now_; asm volatile("s_memrealtime %0\n\ts_waitcnt lgkmcnt(0)" : "=s"(now_)); if ((int)((unsigned)now_ - tt_) >= 0) break; __builtin_amdgcn_s_sleep(1); } } while (0)
  if (b == 0) {
    const u64 pa = wsa + WS_PR + w * 256 + (w & 1) * 8192, pb = pa ^ 8192;
    u64 ta, tb, tc; unsigned da;
    asm volatile("s_memtime %0\n\ts_load_dword %3, %4, 0x0 glc\n\ts_waitcnt lgkmcnt(0)\n\ts_memtime %1\n\ts_load_dword %3, %5, 0x0 glc\n\ts_waitcnt lgkmcnt(0)\n\ts_memtime %2\n\ts_waitcnt lgkmcnt(0)"
                 : "=&s"(ta), "=&s"(tb), "=&s"(tc), "=&s"(da) : "s"(pa), "s"(pb));
    int d = (int)(2 * (unsigned)tb - (unsigned)ta - (unsigned)tc);
    d = d > 400 ? 400 : (d < -400 ? -400 : d);
    stl<int>(L + L_X + w * 4, (w & 1) ? -d : d);
    bar();
    const v4u u0 = ldl<v4u>(L + L_X), u1 = ldl<v4u>(L + L_X + 16);
    const int tot = (int)((u0[0] + u0[1]) + (u0[2] + u0[3]) + (u1[0] + u1[1]) + (u1[2] + u1[3]));
    unsigned xcc; asm volatile("s_getreg_b32 %0, hwreg(HW_REG_XCC_ID, 0, 3)" : "=s"(xcc));
    const unsigned pw = ((epoch + 1u) & TMASK) | ((tot > 0 ? 8192u : 0u) ^ (xcc & 4) << 11) | xcc << 16;
    stsc2(v2u{pw, pw}, rws, WS_PH | OFFIF(tid), 0);
  }
  v4u se, so; make_sel(lane, isB || (((lane >> 3) & 1) == b4), se, so);
  asm volatile("" : "+v"(xs0), "+v"(xs1), "+v"(epoch), "+v"(se), "+v"(so));
  SB(); const v4u Y0 = ldnt(r1, soff, o4); SB();
  const unsigned tag = (epoch + 1u) & TMASK;
  float mean = ln_mean(xs0, xs1);
  PIN(mean);
  SB(); const v4u Y1 = ldnt(r2, soff, o5); SB();
  xo = v4u{__builtin_amdgcn_alignbit(xo[1], xo[0], xsf), __builtin_amdgcn_alignbit(xo[2], xo[1], xsf), __builtin_amdgcn_alignbit(xo[3], xo[2], xsf), __builtin_amdgcn_alignbit(xo[3], xo[3], xsf)};
  const float inv = ln_inv(xs0, xs1, mean), mb = rbf(mean);
  v4u xn = {ln2(xo[0], mb, inv), ln2(xo[1], mb, inv), ln2(xo[2], mb, inv), ln2(xo[3], mb, inv)};
  PIN(xn);
  USE(cpf);
  SB(); const v4u Z0 = ldnt(r1x, soff, 6 * 8192); SB();
  v4f z4 = {0.f, 0.f, 0.f, 0.f};
#define Q_ADDR(HEAD0, PART) \
  unsigned a_rw = L + L_RED + (w * 129 + b5 * 32 + n * 2 + b4) * 16, a_rr = L + L_RED + ((lane >> 3) * 129 + w * 8 + c8) * 16; \
  unsigned o_qp = (WS_QP + (((HEAD0) + (w >> 2)) * 9 + (PART)) * 512 + (w & 3) * 128 + c8 * 16 + b5 * 8) | OFFIF(lane & 24); \
  asm volatile("" : "+v"(a_rw), "+v"(a_rr), "+v"(o_qp));
#define Q_PUBLISH() do { \
      stl<v4f>(a_rw, acc); \
      bar(); \
      const v4f t_ = ldl<v4f>(a_rr); \
      float t0_ = swap32_add(t_[0], t_[2]), t1_ = swap32_add(t_[1], t_[3]); \
      t0_ = swap16_add(t0_, t0_); t1_ = swap16_add(t1_, t1_); \
      t0_ += dpp<ROR8>(t0_); t1_ += dpp<ROR8>(t1_); \
      stsc2(v2u{tagw(t0_, tag), asu(t1_)}, rws, o_qp, 0); } while (0)

  if (isB) {
    S[2] = Y0; S[3] = Y1;
    const rsrc_t rd = {ka[14], ka[15], D * DF * 2, fmt};
    unsigned doff, o_gp, o_gg, o_fp;
    v4u Dn[8];
    int rw;
#define B_LD(k) do { if ((k) < PACE) AT(PT0 + ((k) - 4) * PTD); if ((k) < 16) { SB(); S[(k) & 15] = ldnt(((k) & 1) ? r2 : r1, soff, ((k) >> 1) * (DF * 2)); SB(); } \
      else if ((k) < 24) { SB(); if ((k) == 16) __builtin_amdgcn_s_barrier(); Dn[(k) & 7] = ldnt(rd, doff, (((k) + rw) & 7) * 2048); SB(); } } while (0)
    unsigned a_hw, a_hr = L + L_X + (b4 * 4 + ((lane >> 3) & 1)) * 16;
    unsigned sm0, sm1; int smi;
    SM_SETUP();
    asm volatile("" : "+v"(sm0), "+v"(sm1), "+v"(smi));
    asm volatile("" : "+v"(a_hr));
    v2u PH = {0u, 0u};
    v4f ag = {0.f, 0.f, 0.f, 0.f}, au = {0.f, 0.f, 0.f, 0.f};
#pragma unroll
    for (int i = 0; i < 8; ++i) {
      const v4u A = diag(xn, i, se, so);
      ag = mfma32(A, S[2 * i], ag); PIN(ag); B_LD(2 * i + WB);
      au = mfma32(A, S[2 * i + 1], au); PIN(au); B_LD(2 * i + 1 + WB);
      if (i == 3) { SB(); PH = ldsc2(rws, WS_PH, 0); SB(); }
      if (i == 5) {
        RETRY(PH = ldsc2(rws, WS_PH, 0), BAD2(PH));
        const unsigned pw = __builtin_amdgcn_readfirstlane(PH[0]), fl = pw & 8192, k0 = pw >> 16 & 7;
        const int xv = (xl + k0) & 7, ch = xv >> 2, di = (xv & 3) * 14 + j - 16, gd = di >> 3, rq = di & 7;
#define GLN(gr, m) ((WS_GU + (((((gr) + ((gr) >= 3) + k0) & 3) * 16 + (m)) >> 1) * 16384 + ((((gr) + ((gr) >= 3) + k0) >> 2) & 1) * 8192 + ((m) & 1) * 4096) ^ fl)
        doff = (gd * 512 + rq * 64 + (lane >> 3) * 8) * 2048 + ch * 1024 + w * 128 + c8 * 16;
        o_gp = (GLN(grp, mi) + w * 512 + (c8 >> 1) * 128 + b5 * 64 + (c8 & 1) * 32 + b4 * 16) | OFFIF(lane & 8);
        o_gg = GLN(gd, lane >> 3) + rq * 512 + (w & 3) * 128 + c8 * 16;
        o_fp = FP_OFF(ch, 64 + di) ^ fl;
        rw = rq;
        a_hw = L + L_X + ((lane & 4) ? 128 : 0) + ((w & 3) * 16 + (lane & 2) * 4 + (((lane & 1) * 4 + e4 - rw) & 7)) * 2;
        asm volatile("" : "+v"(doff), "+v"(o_gp), "+v"(o_gg), "+v"(o_fp), "+v"(a_hw)); SB();
      }
    }
    USE(T2); USE(T3); USE(T7); USE(Z0); PIN(z4);
    {
      v4f o;
#pragma unroll
      for (int k = 0; k < 4; ++k) { auto t = __builtin_amdgcn_permlane32_swap(asu(ag[k]), asu(au[k]), false, false); o[k] = asf(t[0]) + dpp<ROR8>(asf(t[1])); }
      stsc(tag4(o, tag), rws, o_gp, 0);
    }
#define G_GATHER() do { SB(); G0 = ldsc(rws, o_gg, 0); G1 = ldsc(rws, o_gg, 4 * 16384); SB(); } while (0)
#define D_REST() do { __builtin_amdgcn_s_barrier(); SB(); _Pragma("unroll") for (int i_ = DPR; i_ < 8; ++i_) Dn[i_] = ldnt(rd, doff, ((i_ + rw) & 7) * 2048); SB(); } while (0)
#pragma unroll
    for (int u = 0; u + WB < DPR; ++u) { USE(Dn[u]); B_LD(16 + WB + u); }
    if (w < 4) {
      v4u G0, G1;
      USE(Dn[DPR - 1]);
      G_GATHER();
      D_REST();
#define H_CALC() do { \
      float t = tr4(unt4(G0) + unt4(G1)); \
      t += dpp<ROR8>(t); \
      t = rbf(t); \
      const float gq = t, uq = dpp<ROR4>(t);        \
      const float inner = 0.7978845608028654f * (gq + 0.044715f * gq * gq * gq); \
        \
      const float sg = __builtin_amdgcn_rcpf(1.f + __builtin_amdgcn_exp2f(inner * (-2.f * LOG2E))); \
      const float hv = rbf(gq * sg) * uq; \
      stl<u16>(a_hw, (u16)pk(hv, hv)); SB(); } while (0)
      unsigned gb = bad4(G0, tag) | bad4(G1, tag);
      PIN(gb); SB();
      H_CALC();
      if (__builtin_expect(!allok(gb), 0)) {
#pragma nounroll
        for (unsigned spin = 0; spin < SPIN_CAP; ++spin) {
          asm volatile("" ::: "memory"); G_GATHER();
          if (allok(bad4(G0, tag) | bad4(G1, tag))) break;
          __builtin_amdgcn_s_sleep(1);
        }
        H_CALC();
      }
    } else {
      D_REST();
    }
    bar();
    gemv64(ldl<v4u>(a_hr), ldl<v4u>(a_hr + 32), sm0, sm1, smi, Dn, tag, rws, o_fp, z4);
  } else if (isA) {
    S[2] = T2; S[3] = T3; S[4] = Y0; S[5] = Y1; S[6] = Z0;
    const rsrc_t rk = {ka[2], ka[3], 4u * SK * 256, fmt}, rv = {ka[4], ka[5], 4u * SK * 256, fmt}, ro = {ka[8], ka[9], 4096 * D * 2, fmt};
    const unsigned kvoff = (unsigned)(g * SK + (SK - WIN) + s * 128 + w * 16) * 256u + lane * 16;
    Q_ADDR(g * 8 + cgi * 2, part)
#define A_LD(k) do { if ((k) >= 7 && (k) < 15) { SB(); if ((k) == 7) __builtin_amdgcn_s_barrier(); \
      S[((k) + 1) & 15] = ldnt(((k) & 1) ? rk : rv, kvoff, (((((k) - 7) >> 1) + (((k) & 1) ? 0 : 2) + s) & 3) * 1024); SB(); } } while (0)
    const int rw = s & 7;
    v4u Wo[8], P[5], one = {0x3f803f80u, 0x3f803f80u, 0x3f803f80u, 0x3f803f80u};
    unsigned sm0, sm1; int smi;
    unsigned ooff, a_kw, a_kr, a_vw, a_vr, a_qw, a_qr, a_mw, a_mr, a_pw, a_pr, o_op, o_ml, o_qg, o_q8, o_og, o_mg, a_xw, a_xr, o_fp;
#define OLN(i) (WS_OP + (((i) & 2047) >> 6) * 16384 + ((i) >> 11) * 8192 + ((i) & 63) * 128)
#define MLN(i) (WS_ML + ((i) >> 6) * 8192 + ((i) & 63) * 128)
    v2u PH;
#define WO_RANGE(lo, hi) do { SB(); _Pragma("unroll") for (int i_ = (lo); i_ < (hi); ++i_) Wo[i_] = ldnt(ro, ooff, ((i_ + rw) & 7) * 2048); SB(); } while (0)
    {
      v4f acc = {0.f, 0.f, 0.f, 0.f};
#define A_STEP(i) do { acc = mfma32(diag(xn, i, se, so), S[i], acc); PIN(acc); A_LD((i) + WA); SB(); } while (0)
#define ADDR1() do { \
      a_kw = L + L_K + (w * 16 + hq) * 272 + n * 16; a_kr = L + L_K + (w * 16 + n) * 272 + hq * 16; \
      a_vw = L + L_V + (w * 16 + hq) * 288 + n * 16; a_vr = L + L_V + (hq * 4 + ((lane >> 2) & 3)) * 288 + w * 32 + (lane & 3) * 8; \
      a_qw = L + L_Q + w * 272 + (lane & 31) * 8 + b5 * 4; a_qr = L + L_Q + c8 * 272 + hq * 16; \
      a_mw = L + L_ML + c8 * 48 + w * 4; a_mr = L + L_ML + c8 * 48; \
      a_pw = L + L_P + c8 * 272 + (w >> 1) * 64 + hq * 16 + (w & 1) * 8; a_pr = L + L_P + c8 * 272 + hq * 16; \
      asm volatile("" : "+v"(a_kw), "+v"(a_kr), "+v"(a_vw), "+v"(a_vr), "+v"(a_qw), "+v"(a_qr), "+v"(a_mw), "+v"(a_mr), "+v"(a_pw), "+v"(a_pr)); \
      SB(); } while (0)
#define ADDR2() do { \
      RETRY(PH = ldsc2(rws, WS_PH, 0), BAD2(PH)); \
      const unsigned pw = __builtin_amdgcn_readfirstlane(PH[0]), fl = pw & 8192, k0 = pw >> 16 & 7, gs = (k0 + 1) >> 1; \
      const int xv = (xl + k0) & 7, ch = xv >> 2, pr = (xv & 3) * 4 + (j >> 2), qd = j & 3;     \
      const int gv = (g + gs) & 3, pv = (pr + gs * 4) & 15;                   \
      ooff = ((pr * 2 + b5) * 128 + qd * 32 + ((lane >> 3) & 3) * 8) * 2048 + ch * 1024 + w * 128 + c8 * 16; \
      o_op = ((OLN(((gv * 4 + (c8 >> 1)) * 8 + w) * 32 + s) + (n & 1) * 64 + hq * 16) ^ fl) | OFFIF(lane & 8); \
      o_ml = ((MLN(gv * 32 + s) + c8 * 16) ^ fl) | OFFIF(lane >> 3); \
      o_og = (OLN((pv * 8 + qd * 2 + (w & 1)) * 32 + (lane >> 3)) + c8 * 16) ^ fl; \
      o_mg = (MLN((pv >> 2) * 32 + (lane >> 3) + (lane & 3) * 8) + ((pr & 3) * 2 + (c8 >> 2)) * 16) ^ fl; \
      o_fp = FP_OFF(ch, (xv & 3) * 16 + j) ^ fl; \
      asm volatile("" : "+v"(ooff), "+v"(o_op), "+v"(o_ml), "+v"(o_og), "+v"(o_mg), "+v"(o_fp)); \
      SB(); } while (0)
#define ADDR3() do { \
      SM_SETUP(); \
      a_xw = L + L_X + ((c8 >> 2) * 32 + (w & 1) * 16 + (lane & 2) * 4 + (((lane & 1) * 4 + e4 - rw) & 7)) * 2; a_xr = L + L_X + (b4 * 4 + ((lane >> 3) & 1)) * 16; \
      o_qg = WS_QP + (g * 8 + w) * 4608 + lane * 16; o_q8 = WS_QP + (g * 8 + w) * 4608 + 4096 + (lane & 31) * 16; \
      asm volatile("" : "+v"(sm0), "+v"(sm1), "+v"(smi)); \
      asm volatile("" : "+v"(a_xw), "+v"(a_xr), "+v"(o_qg), "+v"(o_q8), "+v"(one)); \
      SB(); } while (0)
      A_STEP(0); A_STEP(1); A_STEP(2); A_STEP(3);
      ADDR1();
      A_STEP(4); A_STEP(5); A_STEP(6);
      USE(T7);
      Q_PUBLISH();
      PH = ldsc2(rws, WS_PH, 0);
      SB();
      ADDR3();
    }
#define Q_GATHER() do { SB(); _Pragma("unroll") for (int i_ = 0; i_ < 4; ++i_) P[i_] = ldsc(rws, o_qg, i_ * 1024); P[4] = ldsc(rws, o_q8, 0); SB(); } while (0)
#define Q_BAD() ((bad4(P[0], tag) | bad4(P[1], tag)) | (bad4(P[2], tag) | bad4(P[3], tag)) | bad4(P[4], tag))
#pragma unroll
    for (int u = 0; u < 8; ++u) {
      if (u & 1) stl<v4u>(a_vw + (((u >> 1) + 2) & 3) * 4 * 288, S[8 + u]); else stl<v4u>(a_kw + (u >> 1) * 4 * 272, S[8 + u]);
      A_LD(7 + u + WA);
      if (u == 3) { SB(); ADDR2(); }
      if (u == QAT) { Q_GATHER(); __builtin_amdgcn_s_barrier(); }
      if (u == QAT + 1) WO_RANGE(0, 1);
    }
    ldswait();
    v4u Kf[4];
#pragma unroll
    for (int u = 0; u < 4; ++u) Kf[u] = ldl<v4u>(a_kr + u * 64);
#define Q_SUM() do { const v4f t = (unt4(P[0]) + unt4(P[1])) + (unt4(P[2]) + (0.5f * unt4(P[4]) + unt4(P[3]))); \
      stl<unsigned>(a_qw, pk(swap32_add(t[0], t[2]), swap32_add(t[1], t[3]))); SB(); } while (0)
    unsigned qb = Q_BAD();
    PIN(qb); SB();
    Q_SUM();
    if (__builtin_expect(!allok(qb), 0)) {
#pragma nounroll
      for (unsigned spin = 0; spin < SPIN_CAP; ++spin) {
        asm volatile("" ::: "memory"); Q_GATHER();
        if (allok(Q_BAD())) break;
        __builtin_amdgcn_s_sleep(1);
      }
      Q_SUM();
    }
    WO_RANGE(1, 2);
    bar();
    v4u qf[4];
#pragma unroll
    for (int u = 0; u < 4; ++u) qf[u] = ldl<v4u>(a_qr + u * 64);
    SB();
    v4f sc = mfma32(Kf[0], qf[0], v4f{0.f, 0.f, 0.f, 0.f});
    sc = mfma32(Kf[1], qf[1], sc); sc = mfma32(Kf[2], qf[2], sc); sc = mfma32(Kf[3], qf[3], sc);
    SB();
    v2u Vt[8];
#pragma unroll
    for (int t = 0; t < 8; ++t) Vt[t] = __builtin_bit_cast(v2u, __builtin_amdgcn_ds_read_tr16_b64_v4i16((LAS v4s*)(size_t)(a_vr + t * 16 * 288)));
    SB();
    const float SCALE = 0.08838834764831845f;
    const unsigned s01 = pk(sc[0], sc[1]), s23 = pk(sc[2], sc[3]);
    const unsigned t01 = pk(blo(s01) * SCALE, bhi(s01) * SCALE), t23 = pk(blo(s23) * SCALE, bhi(s23) * SCALE);
    const float s0 = blo(t01), s1_ = bhi(t01), s2_ = blo(t23), s3 = bhi(t23);
    stl<float>(a_mw, swap32_max(swap16_max(fmx(fmx3(s0, s1_, s2_), s3))));
    bar();
    const v4f m0 = ldl<v4f>(a_mr), m1 = ldl<v4f>(a_mr + 16);
    const float M = fmx(fmx3(fmx3(m0[0], m0[1], m0[2]), m0[3], m1[0]), fmx3(m1[1], m1[2], m1[3]));
    {
      const float nm = M * -LOG2E;
      const float p0 = __builtin_amdgcn_exp2f(fmaf(s0, LOG2E, nm)), p1 = __builtin_amdgcn_exp2f(fmaf(s1_, LOG2E, nm));
      const float p2 = __builtin_amdgcn_exp2f(fmaf(s2_, LOG2E, nm)), p3 = __builtin_amdgcn_exp2f(fmaf(s3, LOG2E, nm));
      stl<v2u>(a_pw, v2u{pk(p0, p1), pk(p2, p3)});
    }
    bar();
    {
      v4u pf[4];
#pragma unroll
      for (int u = 0; u < 4; ++u) pf[u] = ldl<v4u>(a_pr + u * 64);
      SB();
#define VT(u) v4u{Vt[2 * (u)][0], Vt[2 * (u)][1], Vt[2 * (u) + 1][0], Vt[2 * (u) + 1][1]}
      v4f oa = mfma32(VT(0), pf[0], v4f{0.f, 0.f, 0.f, 0.f});
      oa = mfma32(VT(1), pf[1], oa); oa = mfma32(VT(2), pf[2], oa); oa = mfma32(VT(3), pf[3], oa);
      stsc(tag4(oa, tag), rws, o_op, 0);
      SB();
      if (w == 3) {
        v4f ls = mfma32(one, pf[0], v4f{0.f, 0.f, 0.f, 0.f});
        ls = mfma32(one, pf[1], ls); ls = mfma32(one, pf[2], ls); ls = mfma32(one, pf[3], ls);
        stsc2(v2u{asu(M) | tag, tagw(ls[0], tag)}, rws, o_ml, 0);
      }
    }
    PIN(z4);
    WO_RANGE(2, WOB);
#define O_GATHER() do { SB(); ML = ldsc2(rws, o_mg, 0); _Pragma("unroll") for (int i_ = 0; i_ < 4; ++i_) PO[i_] = ldsc(rws, o_og, i_ * 1024); SB(); } while (0)
    if (w < 2) {
      v4u PO[4]; v2u ML;
      __builtin_amdgcn_s_sleep(36);
      O_GATHER();
      __builtin_amdgcn_s_barrier(); WO_RANGE(WOB, 8);
      RETRY(O_GATHER(), ((bad4(PO[0], tag) | bad4(PO[1], tag)) | (bad4(PO[2], tag) | bad4(PO[3], tag)) | BAD2(ML)));
      const float m = unt(ML[0]);
      float Mx = fmx(m, dpp<0xB1>(m)); Mx = fmx(Mx, dpp<0x4E>(Mx)); Mx = fmx(Mx, dpp<ROR8>(Mx)); Mx = swap32_max(swap16_max(Mx));
      const float e = __builtin_amdgcn_exp2f((m - Mx) * LOG2E);
      float Ls = e * unt(ML[1]);
      const v4f t = (dpp<0x00>(e) * unt4(PO[0]) + dpp<0x55>(e) * unt4(PO[1])) + (dpp<0xAA>(e) * unt4(PO[2]) + dpp<0xFF>(e) * unt4(PO[3]));
      Ls += dpp<0xB1>(Ls); Ls += dpp<0x4E>(Ls); Ls += dpp<ROR8>(Ls);
      Ls = swap16_add(Ls, Ls); Ls = swap32_add(Ls, Ls);
      float o = tr4(t);
      o += dpp<ROR8>(o);
      o *= __builtin_amdgcn_rcpf(Ls);
      stl<u16>(a_xw, (u16)pk(o, o));
    } else {
      __builtin_amdgcn_s_barrier(); WO_RANGE(WOB, 8);
    }
    bar();
    gemv64(ldl<v4u>(a_xr), ldl<v4u>(a_xr + 32), sm0, sm1, smi, Wo, tag, rws, o_fp, z4);
  } else {
    S[2] = T2; S[3] = T3; S[4] = Y0; S[5] = Y1; S[6] = Z0; S[7] = T7;
    Q_ADDR(r * 2, 8)
    {
      v4f acc = {0.f, 0.f, 0.f, 0.f};
#pragma unroll
      for (int i = 0; i < 8; ++i) { const int u = i < 4 ? i : (i == 4 ? 7 : i - 1); acc = mfma32(diag(xn, u, se, so), S[u], acc); }
      Q_PUBLISH();
    }
    v2u PH = ldsc2(rws, WS_PH, 0);
    RETRY(PH = ldsc2(rws, WS_PH, 0), BAD2(PH));
    const unsigned pw = __builtin_amdgcn_readfirstlane(PH[0]), fl = pw & 8192;
    const int rb = ((xl + (pw >> 16)) & 7) * 2 + jd;
    const unsigned xcol = raw_ld16(rx, (rb * 64 + lane) * 2, 0, 0);
    const int p0 = (w < 4 ? w * 16 : 64 + (w - 4) * 14) + hq;
    const bool last = w < 4 || hq < 2;
#define FP_IN(p) ((WS_FP + ((p) >> 2) * 16384 + (rb >> 3) * 8192 + ((p) & 3) * 2048 + (rb & 7) * 256 + n * 16) ^ fl)
    unsigned o_f = FP_IN(p0), o_f3 = FP_IN(last ? p0 + 12 : p0);
    unsigned a_w = L + L_K + (n * 4 + e4) * 48 + (w & 3) * 8 + (w >> 2) * 4, a_r = L + L_K + lane * 48;
    unsigned o_out = (rb * 64 + lane) * 2, o_ep = (rb == 0 && lane == 0) ? 0 : OOB, ep1 = epoch + 1u;
    float lw = last ? 1.f : 0.f;
    asm volatile("" : "+v"(o_f), "+v"(o_f3), "+v"(a_w), "+v"(a_r), "+v"(lw), "+v"(o_out), "+v"(o_ep), "+v"(ep1));
    float xf = blo(xcol);
    rsrc_t rout = {kb[0], kb[1], szd, fmt};
    asm volatile("" : "+v"(xf), "+s"(rout));
    AT(TRS);
    v4u P0, P1, P2, P3;
    {
#pragma nounroll
      for (unsigned spin = 0; spin < SPIN_CAP; ++spin) {
        asm volatile("" ::: "memory");
        P0 = ldsc(rws, o_f, 0); P1 = ldsc(rws, o_f, 16384); P2 = ldsc(rws, o_f, 32768); P3 = ldsc(rws, o_f3, 0);
        if (allok((bad4(P0, tag) | bad4(P1, tag)) | (bad4(P2, tag) | bad4(P3, tag)))) break;
      }
    }
    v4f t = (unt4(P0) + unt4(P1)) + unt4(P2);
    t = lw * unt4(P3) + t;
    stl<float>(a_w, tr4(t));
    bar();
    if (w == 0) {
      const v4f u = ldl<v4f>(a_r) + ldl<v4f>(a_r + 16);
      const unsigned ab = pk(u[0] + u[2], u[1] + u[3]);
      const float y = rbf(xf + blo(ab)) + bhi(ab);
      raw_st16((u16)pk(y, y), rout, o_out, 0, 0);
      stsc1(ep1, rws, o_ep, 0);
    }
  }
}

extern "C" void kernel_launch(void* const* d_in, const int* in_sizes, int n_in, void* d_out, int out_size, void* d_ws, size_t ws_size,
                              hipStream_t stream) {
  fused_layer<<<256, 512, 0, stream>>>((const u16*)d_in[0], (const u16*)d_in[1], (const u16*)d_in[2], (const u16*)d_in[3], (const u16*)d_in[4],
                                       (const u16*)d_in[6], (const u16*)d_in[7], (const u16*)d_in[8], (u16*)d_out, (unsigned*)d_ws);
}
```
